```python
import torch

def _softmax(x, axis=-1):
    return torch.softmax(x, dim=axis)
def _gelu(x):
    return torch.nn.functional.gelu(x, approximate="tanh")
def _silu(x):
    return torch.nn.functional.silu(x)
def _sigmoid(x):
    return torch.sigmoid(x)
def _layernorm(x, axis=-1, eps=1e-5):
    m = x.mean(dim=axis, keepdim=True)
    v = x.var(dim=axis, keepdim=True, unbiased=False)
    return (x - m) / torch.sqrt(v + eps)
def _scatter_add(tgt, idx, src, axis=0):
    return tgt.clone().index_add_(axis, idx, src)
def _segment_sum(src, seg, n):
    out = torch.zeros((n,) + src.shape[1:], dtype=src.dtype)
    return out.index_add_(0, seg, src)
def _pad(x, pw):
    flat = tuple(v for pair in reversed(pw) for v in pair)
    return torch.nn.functional.pad(x, flat)

kernel_name = 'standalone__softmax_full_bidir__decode'

def setup_inputs():

    _w = (lambda a: (lambda t: t.to(torch.bfloat16) if t.is_floating_point() else t)(torch.as_tensor(a)))
    B, H, Sq, Skv, Dh = 4, 16, 1, 16384, 256
    return {
        "q": _w((lambda *s: torch.randn(*s, dtype=torch.bfloat16) * 0.8)(B, H, Sq, Dh)),
        "k": _w((lambda *s: torch.randn(*s, dtype=torch.bfloat16) * 0.8)(B, H, Skv, Dh)),
        "v": _w((lambda *s: torch.randn(*s, dtype=torch.bfloat16) * 0.05)(B, H, Skv, Dh)),
    }

def reference(q, k, v):
    scale = q.shape[-1] ** -0.5
    scores = torch.einsum('bhqd,bhkd->bhqk', q, k) * scale
    attn = _softmax(scores, axis=-1)
    return torch.einsum('bhqk,bhkd->bhqd', attn, v)
```

```cpp
#include <hip/hip_runtime.h>
#include <hip/hip_bf16.h>
#include <stdint.h>

typedef unsigned u32x4 __attribute__((ext_vector_type(4)));
typedef float f32x4 __attribute__((ext_vector_type(4)));
typedef __bf16 bf16x2 __attribute__((ext_vector_type(2)));

constexpr int D = 256, SKV = 16384, NBH = 64;
constexpr int NSPLIT = 4;
constexpr int NW = 8;
constexpr int RB = 16;
constexpr int NL = RB / 2;
constexpr int KEYS_WG = SKV / NSPLIT;
constexpr int NBLK = KEYS_WG / (RB * NW);
constexpr int REC = D + 4;

__device__ __forceinline__ float dot2(unsigned k, unsigned q, float acc) {
    return __builtin_amdgcn_fdot2_f32_bf16(__builtin_bit_cast(bf16x2, k), __builtin_bit_cast(bf16x2, q), acc, false);
}
template <int CTRL> __device__ __forceinline__ float dpp(float x) {
    return __builtin_bit_cast(float, __builtin_amdgcn_mov_dpp(__builtin_bit_cast(int, x), CTRL, 0xf, 0xf, true));
}
constexpr int XOR1 = 0xB1, XOR2 = 0x4E, XOR7 = 0x141, ROR8 = 0x128, ROR4 = 0x124;
template <int N> __device__ __forceinline__ float bcast_row(float x) { return dpp<0x150 + N>(x); }
__device__ __forceinline__ float u2f(unsigned u) { return __builtin_bit_cast(float, u); }
__device__ __forceinline__ unsigned f2u(float f) { return __builtin_bit_cast(unsigned, f); }

struct State { float m, l, acc[8]; };

__device__ __forceinline__ void load_blk(u32x4 (&buf)[NL], __amdgpu_buffer_rsrc_t rs, int voff, int soff) {
#pragma unroll
    for (int t = 0; t < NL; ++t)
    {
        buf[t] = __builtin_amdgcn_raw_buffer_load_b128(rs, voff + (t & 3) * 1024, soff + (t >> 2) * 4096, 2);
        __builtin_amdgcn_sched_barrier(0);
    }
}

__device__ __forceinline__ float scores(const u32x4 (&K)[NL], const u32x4& q, int lane) {
    float x[NL];
#pragma unroll
    for (int t = 0; t < NL; ++t) {
        float d = dot2(K[t][0], q[0], 0.f);
        d = dot2(K[t][1], q[1], d);
        d = dot2(K[t][2], q[2], d);
        x[t] = dot2(K[t][3], q[3], d);
    }
    float y[4];
#pragma unroll
    for (int t = 0; t < 4; ++t) {
        auto s = __builtin_amdgcn_permlane16_swap(f2u(x[t]), f2u(x[t + 4]), false, false);
        y[t] = u2f(s[0]) + u2f(s[1]);
    }
    float z[2];
#pragma unroll
    for (int t = 0; t < 2; ++t) {
        const float a = y[t] + dpp<ROR8>(y[t]), b = y[t + 2] + dpp<ROR8>(y[t + 2]);
        z[t] = (lane & 8) ? b : a;
    }
    const float a = z[0] + dpp<XOR7>(z[0]), b = z[1] + dpp<XOR7>(z[1]);
    float s = (lane & 4) ? b : a;
    s += dpp<XOR1>(s);
    s += dpp<XOR2>(s);
    return s;
}

__device__ __forceinline__ void pv_row(State& st, const u32x4& v, float p) {
#pragma unroll
    for (int k = 0; k < 4; ++k) {
        st.acc[2 * k] = fmaf(p, u2f(v[k] << 16), st.acc[2 * k]);
        st.acc[2 * k + 1] = fmaf(p, u2f(v[k] & 0xffff0000u), st.acc[2 * k + 1]);
    }
}

__device__ __forceinline__ float softmax_step(State& st, float s) {
    constexpr float C2 = 0.0625f * 1.4426950408889634f;
    s *= C2;
    float pm = fmaxf(s, dpp<ROR4>(s));
    pm = fmaxf(pm, dpp<ROR8>(pm));
    auto sw = __builtin_amdgcn_permlane16_swap(f2u(pm), f2u(pm), false, false);
    pm = fmaxf(u2f(sw[0]), u2f(sw[1]));
    const float mn = fmaxf(st.m, pm);
    const float alpha = __builtin_amdgcn_exp2f(st.m - mn);
    const float p = __builtin_amdgcn_exp2f(s - mn);
    st.m = mn;
    st.l = fmaf(st.l, alpha, p);
#pragma unroll
    for (int k = 0; k < 8; ++k) st.acc[k] *= alpha;
    return p;
}

__device__ __forceinline__ void pv(State& st, const u32x4 (&V)[NL], float p) {
    auto sw = __builtin_amdgcn_permlane16_swap(f2u(p), f2u(p), false, false);
    const float pa = u2f(sw[0]), pb = u2f(sw[1]);
    pv_row(st, V[0], bcast_row<0>(pa));
    pv_row(st, V[1], bcast_row<4>(pa));
    pv_row(st, V[2], bcast_row<8>(pa));
    pv_row(st, V[3], bcast_row<12>(pa));
    pv_row(st, V[4], bcast_row<0>(pb));
    pv_row(st, V[5], bcast_row<4>(pb));
    pv_row(st, V[6], bcast_row<8>(pb));
    pv_row(st, V[7], bcast_row<12>(pb));
}

__global__ void __launch_bounds__(NW * 64, 2)
attn_split(const uint16_t* __restrict__ q, const uint16_t* __restrict__ k, const uint16_t* __restrict__ v,
           float* __restrict__ part) {
    __shared__ __attribute__((aligned(16))) float lds[NW * 2 * REC];
    const int bh = blockIdx.x / NSPLIT, sp = blockIdx.x % NSPLIT;
    const int tid = threadIdx.x, lane = tid & 63, wave = __builtin_amdgcn_readfirstlane(tid >> 6);
    const size_t base = ((size_t)bh * SKV + (size_t)sp * KEYS_WG) * D;
    const __amdgpu_buffer_rsrc_t rk = __builtin_amdgcn_make_buffer_rsrc((void*)(k + base), 0, KEYS_WG * D * 2, 0x00020000);
    const __amdgpu_buffer_rsrc_t rv = __builtin_amdgcn_make_buffer_rsrc((void*)(v + base), 0, KEYS_WG * D * 2, 0x00020000);
    const int voff = lane * 16;
    constexpr int BLK_BYTES = RB * D * 2, STEP = NW * BLK_BYTES;
    int soff = wave * BLK_BYTES;

    const u32x4 qv = *reinterpret_cast<const u32x4*>(q + (size_t)bh * D + (lane & 31) * 8);
    u32x4 K0[NL], V0[NL], K1[NL], V1[NL];
    __builtin_amdgcn_sched_barrier(0);
    load_blk(K0, rk, voff, soff);
    __builtin_amdgcn_sched_barrier(0);
    load_blk(V0, rv, voff, soff);
    __builtin_amdgcn_sched_barrier(0);
    load_blk(K1, rk, voff, soff + STEP);
    __builtin_amdgcn_sched_barrier(0);
    load_blk(V1, rv, voff, soff + STEP);
    __builtin_amdgcn_sched_barrier(0);
    State st;
    st.m = -INFINITY; st.l = 0.f;
#pragma unroll
    for (int i = 0; i < 8; ++i) st.acc[i] = 0.f;

    for (int i = 0; i < NBLK - 2; i += 2) {
        soff += 2 * STEP;
        float s = scores(K0, qv, lane);
        __builtin_amdgcn_sched_barrier(0);
        load_blk(K0, rk, voff, soff);
        float p = softmax_step(st, s);
        pv(st, V0, p);
        __builtin_amdgcn_sched_barrier(0);
        load_blk(V0, rv, voff, soff);
        s = scores(K1, qv, lane);
        __builtin_amdgcn_sched_barrier(0);
        load_blk(K1, rk, voff, soff + STEP);
        p = softmax_step(st, s);
        pv(st, V1, p);
        __builtin_amdgcn_sched_barrier(0);
        load_blk(V1, rv, voff, soff + STEP);
    }
    {
        float s = scores(K0, qv, lane);
        float p = softmax_step(st, s);
        pv(st, V0, p);
        __builtin_amdgcn_sched_barrier(0);
        s = scores(K1, qv, lane);
        p = softmax_step(st, s);
        pv(st, V1, p);
    }
    float l = st.l;
    l += dpp<ROR4>(l);
    l += dpp<ROR8>(l);
    {
        auto sw = __builtin_amdgcn_permlane16_swap(f2u(l), f2u(l), false, false);
        l = u2f(sw[0]) + u2f(sw[1]);
    }
    float* rec = lds + (wave * 2 + (lane >> 5)) * REC;
    *reinterpret_cast<f32x4*>(rec + (lane & 31) * 8) = f32x4{st.acc[0], st.acc[1], st.acc[2], st.acc[3]};
    *reinterpret_cast<f32x4*>(rec + (lane & 31) * 8 + 4) = f32x4{st.acc[4], st.acc[5], st.acc[6], st.acc[7]};
    if ((lane & 31) == 0) { rec[D] = st.m; rec[D + 1] = l; }
    __syncthreads();
    if (tid < D) {
        float M = -INFINITY;
#pragma unroll
        for (int r = 0; r < NW * 2; ++r) M = fmaxf(M, lds[r * REC + D]);
        float L = 0.f, o = 0.f;
#pragma unroll
        for (int r = 0; r < NW * 2; ++r) {
            const float w = __builtin_amdgcn_exp2f(lds[r * REC + D] - M);
            L = fmaf(w, lds[r * REC + D + 1], L);
            o = fmaf(w, lds[r * REC + tid], o);
        }
        float* dst = part + (size_t)blockIdx.x * REC;
        dst[tid] = o;
        if (tid == 0) { dst[D] = M; dst[D + 1] = L; }
    }
}

__global__ void __launch_bounds__(D) attn_combine(const float* __restrict__ part, __hip_bfloat16* __restrict__ out) {
    const int bh = blockIdx.x, tid = threadIdx.x;
    const float* p = part + (size_t)bh * NSPLIT * REC;
    float M = -INFINITY;
#pragma unroll
    for (int s = 0; s < NSPLIT; ++s) M = fmaxf(M, p[s * REC + D]);
    float L = 0.f, o = 0.f;
#pragma unroll
    for (int s = 0; s < NSPLIT; ++s) {
        const float w = __builtin_amdgcn_exp2f(p[s * REC + D] - M);
        L = fmaf(w, p[s * REC + D + 1], L);
        o = fmaf(w, p[s * REC + tid], o);
    }
    out[bh * D + tid] = __float2bfloat16(o / L);
}

extern "C" void kernel_launch(void* const* d_in, const int* in_sizes, int n_in,
                              void* d_out, int out_size, void* d_ws, size_t ws_size,
                              hipStream_t stream) {
    const uint16_t* q = (const uint16_t*)d_in[0];
    const uint16_t* k = (const uint16_t*)d_in[1];
    const uint16_t* v = (const uint16_t*)d_in[2];
    float* part = (float*)d_ws;
    attn_split<<<NBH * NSPLIT, NW * 64, 0, stream>>>(q, k, v, part);
    attn_combine<<<NBH, D, 0, stream>>>(part, (__hip_bfloat16*)d_out);
}
```

```cpp
#include <hip/hip_runtime.h>
#include <hip/hip_bf16.h>
#include <stdint.h>

typedef unsigned u32x4 __attribute__((ext_vector_type(4)));
typedef unsigned u32x2 __attribute__((ext_vector_type(2)));
typedef int i32x4 __attribute__((ext_vector_type(4)));
typedef float f32x4 __attribute__((ext_vector_type(4)));
typedef float f32x2 __attribute__((ext_vector_type(2)));
typedef __bf16 bf16x2 __attribute__((ext_vector_type(2)));
#define LAS __attribute__((address_space(3)))

constexpr int D = 256, SKV = 16384, NBH = 64;
constexpr int W_NEAR = 19, W_NEAR2 = 18, W_FAR = 22, W_ANY = 20;
constexpr int N1 = 8;
constexpr int WIN0 = 10, NWIN = 5;
constexpr int XR = 36;
constexpr int LPER = 4, PH_LD = 2, PH_USE = 4;
constexpr float TH1 = 20.f, TH2 = 60.f, TH3 = 100.f, TAU0 = 106.f;
constexpr int END_R = 112;
constexpr float TE1 = 12.f, TE2 = 35.f, BIAS = 10.f;
constexpr int EMIN = 1200, T_L2 = 330;
constexpr int NSPLIT = 4;
constexpr int NC = 4;
constexpr int ROUND = 32768;
constexpr int NR = SKV / NSPLIT / 64;
constexpr int NBUF = 3;
constexpr int SC_OFF = NBUF * ROUND;
constexpr int MX_OFF = SC_OFF + NC * 1024 * 4;
constexpr int SEL_OFF = MX_OFF + 48;
constexpr int LDS_BYTES = MX_OFF + 64;
constexpr int REC_BYTES = 1280, HEAD_BYTES = NSPLIT * REC_BYTES;
constexpr int HS_OFF = NBH * HEAD_BYTES, HS_BYTES = 512;
constexpr int NPUB = 16, PUB_STRIDE = 256;
constexpr int TB_OFF = HS_OFF + NBH * HS_BYTES, TB_BYTES = ((2 * NR) >> LPER) * NPUB * PUB_STRIDE;
constexpr int EV_OFF = TB_OFF + TB_BYTES;
constexpr int SX_OFF = EV_OFF + 16 * PUB_STRIDE, SX_BYTES = N1 * 64 * 4;
constexpr int WS_BYTES = SX_OFF + NBH * NSPLIT * SX_BYTES;
static_assert(SX_OFF % (2 * SX_BYTES) == 0 && N1 == 8, "partner box = box ^ SX_BYTES; 8 early scores per lane");
constexpr unsigned EMPTY = 0xffffffffu;

__device__ __forceinline__ float dot2(unsigned k, unsigned q, float acc) {
    return __builtin_amdgcn_fdot2_f32_bf16(__builtin_bit_cast(bf16x2, k), __builtin_bit_cast(bf16x2, q), acc, false);
}
template <int CTL> __device__ __forceinline__ int dppi(int x) { return __builtin_amdgcn_mov_dpp(x, CTL, 0xf, 0xf, true); }
template <int CTL> __device__ __forceinline__ float dpp(float x) {
    return __builtin_bit_cast(float, dppi<CTL>(__builtin_bit_cast(int, x)));
}
constexpr int XOR1 = 0xB1, XOR2 = 0x4E, XOR7 = 0x141, ROR8 = 0x128, ROR4 = 0x124;
__device__ __forceinline__ float u2f(unsigned u) { return __builtin_bit_cast(float, u); }
__device__ __forceinline__ unsigned f2u(float f) { return __builtin_bit_cast(unsigned, f); }
__device__ __forceinline__ void barrier() { asm volatile("s_waitcnt lgkmcnt(0)\n\ts_barrier" ::: "memory"); }

template <bool EQ> __device__ __forceinline__ void barrier_if(int x, int y) {
    if (EQ) asm volatile("s_cmp_lg_u32 %0, %1\n\ts_cbranch_scc1 1f\n\ts_waitcnt lgkmcnt(0)\n\ts_barrier\n1:" :: "s"(x), "n"(y) : "memory", "scc");
    else asm volatile("s_cmp_eq_u32 %0, %1\n\ts_cbranch_scc1 1f\n\ts_waitcnt lgkmcnt(0)\n\ts_barrier\n1:" :: "s"(x), "n"(y) : "memory", "scc");
}

__device__ __forceinline__ float wave_max(float x) {
    x = fmaxf(x, dpp<XOR1>(x));
    x = fmaxf(x, dpp<XOR2>(x));
    x = fmaxf(x, dpp<ROR4>(x));
    x = fmaxf(x, dpp<ROR8>(x));
    auto s = __builtin_amdgcn_permlane16_swap(f2u(x), f2u(x), false, false);
    x = fmaxf(u2f(s[0]), u2f(s[1]));
    auto t = __builtin_amdgcn_permlane32_swap(f2u(x), f2u(x), false, false);
    return fmaxf(u2f(t[0]), u2f(t[1]));
}
__device__ __forceinline__ float wave_sum(float x) {
    x += dpp<XOR1>(x);
    x += dpp<XOR2>(x);
    x += dpp<ROR4>(x);
    x += dpp<ROR8>(x);
    auto s = __builtin_amdgcn_permlane16_swap(f2u(x), f2u(x), false, false);
    x = u2f(s[0]) + u2f(s[1]);
    auto t = __builtin_amdgcn_permlane32_swap(f2u(x), f2u(x), false, false);
    return u2f(t[0]) + u2f(t[1]);
}
__device__ __forceinline__ int row_sum(int x) {
    x += dppi<XOR1>(x);
    x += dppi<XOR2>(x);
    x += dppi<ROR4>(x);
    x += dppi<ROR8>(x);
    return x;
}

__device__ __forceinline__ float scores(const u32x4 (&K)[8], const u32x4& q, int lane) {
    float x[8];
#pragma unroll
    for (int t = 0; t < 8; ++t) {
        float d = dot2(K[t][0], q[0], 0.f);
        d = dot2(K[t][1], q[1], d);
        d = dot2(K[t][2], q[2], d);
        x[t] = dot2(K[t][3], q[3], d);
    }
    float y[4];
#pragma unroll
    for (int t = 0; t < 4; ++t) {
        auto s = __builtin_amdgcn_permlane16_swap(f2u(x[t]), f2u(x[t + 4]), false, false);
        y[t] = u2f(s[0]) + u2f(s[1]);
    }
    float z[2];
#pragma unroll
    for (int t = 0; t < 2; ++t) {
        const float a = y[t] + dpp<ROR8>(y[t]), b = y[t + 2] + dpp<ROR8>(y[t + 2]);
        z[t] = (lane & 8) ? b : a;
    }
    const float a = z[0] + dpp<XOR7>(z[0]), b = z[1] + dpp<XOR7>(z[1]);
    float s = (lane & 4) ? b : a;
    s += dpp<XOR1>(s);
    s += dpp<XOR2>(s);
    return s;
}

__device__ __forceinline__ i32x4 make_rsrc(const void* p, int bytes) {
    const unsigned long long a = (unsigned long long)p;
    return i32x4{(int)(unsigned)a, (int)((unsigned)(a >> 32) & 0xffffu), bytes, 0x00020000};
}

#define GROUP4(W)                                                                                            \
    "s_waitcnt vmcnt(" #W "-1)\n\tbuffer_load_dwordx4 %[voff], s[40:43], s49 offen nt lds\n\t"               \
    "s_add_u32 s46, s46, 1\n\t"                                                                              \
    "s_bitcmp1_b32 s46, 1\n\t"                                                                               \
    "s_cselect_b32 s47, s52, s53\n\t"                                                                        \
    "s_waitcnt vmcnt(" #W "-1)\n\tbuffer_load_dwordx4 %[voff], s[40:43], s49 offen offset:1024 nt lds\n\t"   \
    "s_bitcmp1_b32 s46, 0\n\t"                                                                               \
    "s_cselect_b32 s47, s51, s47\n\t"                                                                        \
    "s_waitcnt vmcnt(" #W "-1)\n\tbuffer_load_dwordx4 %[voff], s[40:43], s49 offen offset:2048 nt lds\n\t"   \
    "s_add_u32 s44, s44, 0x1000\n\t"                                                                         \
    "s_waitcnt vmcnt(" #W "-1)\n\tbuffer_load_dwordx4 %[voff], s[40:43], s49 offen offset:3072 nt lds\n\t"   \
    "s_xor_b32 s49, s49, s47\n\t"
#define ROUND_LOOP(W)                                                                                        \
    ".Lattn_w" #W ":\n\t"                                                                                    \
    "s_mov_b32 m0, s44\n\t"                                                                                  \
    GROUP4(W)                                                                                                \
    "s_and_b32 s47, s46, 7\n\t"                                                                              \
    "s_cbranch_scc0 2f\n\t"                                                                                  \
    "s_cmp_lt_u32 s47, 6\n\t"                                                                                \
    "s_cbranch_scc1 .Lattn_w" #W "\n\t"                                                                      \
    "s_cmp_eq_u32 s47, 7\n\t"                                                                                \
    "s_cbranch_scc1 1f\n\t"                                                                                  \
    "s_barrier\n\t"                                                                                          \
    "s_or_b32 s67, s67, 0x200000\n\t"                                                                        \
    "s_add_u32 s67, s67, 0x10000\n\t"                                                                        \
    "s_branch .Lattn_w" #W "\n"                                                                              \
    "1:\n\t"                                                                                                 \
    "ds_read_b32 v2, v3\n\t"                                                                                 \
    "s_and_b32 s66, s67, 0x5f0000\n\t"                                                                       \
    "s_branch .Lattn_w" #W "\n"                                                                              \
    "2:\n\t"                                                                                                 \
    "s_xor_b32 s49, s66, s50\n\t"                                                                            \
    "s_cmp_eq_u32 s44, %[lend]\n\t"                                                                          \
    "s_cselect_b32 s44, %[lbase], s44\n\t"                                                                   \
    "s_waitcnt lgkmcnt(0)\n\t"                                                                               \
    "v_readfirstlane_b32 s48, v2\n\t"                                                                        \
    "s_cmp_eq_u32 s46, s68\n\t"                                                                              \
    "s_cbranch_scc1 .Lattn_event\n\t"                                                                        \
    "s_cmp_eq_u32 s48, " #W "-16\n\t"                                                                        \
    "s_cbranch_scc1 .Lattn_w" #W "\n\t"                                                                      \
    "s_branch .Lattn_jump\n"

__device__ __forceinline__ void loader(int wave, int lbase, int voff, const void* kp, const void* vp, int base, int prober,
                                       int win8) {
    asm volatile(
        ".Lattn_code_begin:\n\t"
        "s_cmp_lg_u32 %[wave], 0\n\t"
        "s_cbranch_scc1 .Lattn_skip\n\t"
        "s_setprio 3\n\t"
        "s_mov_b64 s[40:41], %[kp]\n\t"
        "s_mov_b32 s42, %[nbytes]\n\t"
        "s_mov_b32 s43, 0x20000\n\t"
        "s_mov_b32 s44, %[lbase]\n\t"
        "s_mov_b32 s46, 0\n\t"
        "s_mov_b32 s50, %[base]\n\t"
        "s_mov_b32 s49, %[base]\n\t"
        "s_mov_b32 s51, 0x8000\n\t"
        "s_mov_b32 s52, 0x9000\n\t"
        "s_mov_b32 s53, 0xd000\n\t"
        "s_mov_b32 s67, 0x10000\n\t"
        "s_movk_i32 s68, %[n1]*8\n\t"
        "v_mov_b32 v3, %[sel]\n\t"
        "s_cmp_eq_u32 %[prober], 0\n\t"
        "s_cbranch_scc1 .Lattn_first\n\t"
        "s_barrier\n"
        ".Lattn_first:\n\t"
        "s_mov_b32 m0, s44\n\t"
        GROUP4(20)
        "s_cmp_lt_u32 s46, 8\n\t"
        "s_cbranch_scc1 .Lattn_first\n\t"
        "s_xor_b32 s49, s67, s50\n\t"
        "s_getpc_b64 s[56:57]\n"
        ".Lattn_pc0:\n\t"
        "s_add_u32 s56, s56, .Lattn_w16-.Lattn_pc0\n\t"
        "s_addc_u32 s57, s57, 0\n\t"
        "s_branch .Lattn_w20\n"
        ".Lattn_event:\n\t"
        "s_cmpk_eq_u32 s46, 1024\n\t"
        "s_cbranch_scc1 .Lattn_drain\n\t"
        "s_cmpk_eq_u32 s46, 512\n\t"
        "s_cbranch_scc0 4f\n\t"
        "s_mov_b64 s[40:41], %[vp]\n\t"
        "s_mov_b32 s67, 0\n\t"
        "s_mov_b32 s49, s50\n\t"
        "s_movk_i32 s68, 1024\n\t"
        "s_branch .Lattn_jump\n"
        "4:\n\t"
        "s_cmpk_eq_u32 s46, %[n1]*8\n\t"
        "s_cbranch_scc1 6f\n\t"
        "s_xor_b32 s50, s50, 0x2000\n\t"
        "s_xor_b32 s49, s49, 0x2000\n\t"
        "s_add_u32 s68, s46, 64\n\t"
        "s_cmp_eq_u32 s46, %[win8]\n\t"
        "s_cselect_b32 s68, s68, 0x200\n\t"
        "s_branch .Lattn_jump\n"
        "6:\n\t"
        "s_mov_b32 s48, 0x400000\n"
        "5:\n\t"
        "ds_read_b64 v[4:5], v3\n\t"
        "s_waitcnt lgkmcnt(0)\n\t"
        "v_readfirstlane_b32 s47, v5\n\t"
        "s_sub_u32 s48, s48, 1\n\t"
        "s_cselect_b32 s47, -1, s47\n\t"
        "s_bitcmp1_b32 s47, 31\n\t"
        "s_cbranch_scc0 5b\n\t"
        "v_readfirstlane_b32 s48, v4\n\t"
        "s_bitcmp1_b32 s47, 4\n\t"
        "s_cselect_b32 s68, %[win8], 0x200\n\t"
        "s_and_b32 s47, s47, 1\n\t"
        "s_lshl_b32 s47, s47, 13\n\t"
        "s_xor_b32 s50, s50, s47\n\t"
        "s_xor_b32 s49, s49, s47\n"
        ".Lattn_jump:\n\t"
        "s_min_u32 s48, s48, 8\n\t"
        "s_mul_i32 s47, s48, .Lattn_w17-.Lattn_w16\n\t"
        "s_add_u32 s54, s56, s47\n\t"
        "s_addc_u32 s55, s57, 0\n\t"
        "s_setpc_b64 s[54:55]\n"
        ROUND_LOOP(16) ROUND_LOOP(17) ROUND_LOOP(18) ROUND_LOOP(19) ROUND_LOOP(20)
        ROUND_LOOP(21) ROUND_LOOP(22) ROUND_LOOP(23) ROUND_LOOP(24)
        ".Lattn_drain:\n\t"
        "s_waitcnt vmcnt(8)\n\t"
        "s_barrier\n\t"
        "s_waitcnt vmcnt(0)\n\t"
        "s_barrier\n\t"
        "s_endpgm\n"
        ".Lattn_skip:"
        :: [wave] "s"(wave), [voff] "v"(voff), [kp] "s"(kp), [vp] "s"(vp), [base] "s"(base), [prober] "s"(prober), [win8] "s"(win8),
           [lbase] "s"(lbase), [lend] "s"(lbase + NBUF * ROUND), [sel] "s"(lbase + SEL_OFF), [nbytes] "n"(SKV * D * 2),
           [n1] "n"(N1)
        : "memory", "scc", "s40", "s41", "s42", "s43", "s44", "s46", "s47", "s48", "s49", "s50", "s51", "s52", "s53",
          "s54", "s55", "s56", "s57", "s66", "s67", "s68", "v2", "v3", "v4", "v5"
        );
}

__device__ __forceinline__ void st16(i32x4 rs, int off, u32x4 x) {
    asm volatile("buffer_store_dwordx4 %0, %1, %2, 0 offen sc1\n\ts_nop 1" :: "v"(x), "v"(off), "s"(rs) : "memory");
}
__device__ __forceinline__ void st8(i32x4 rs, int off, u32x2 x) {
    asm volatile("buffer_store_dwordx2 %0, %1, %2, 0 offen sc1\n\ts_nop 1" :: "v"(x), "v"(off), "s"(rs) : "memory");
}
__device__ __forceinline__ void ld4_issue(i32x4 rs, int off) {
    asm volatile("buffer_load_dword a0, %0, %1, 0 offen sc1" :: "v"(off), "s"(rs) : "memory", "a0");
}
__device__ __forceinline__ unsigned ld4_get() {
    unsigned x;
    asm volatile("s_waitcnt vmcnt(0)\n\tv_accvgpr_read_b32 %0, a0" : "=v"(x) :: "memory", "a0");
    return x;
}
__device__ __forceinline__ void ld8_issue(i32x4 rs, int off) {
    asm volatile("buffer_load_dwordx2 a[2:3], %0, %1, 0 offen sc1" :: "v"(off), "s"(rs) : "memory", "a2", "a3");
}
__device__ __forceinline__ unsigned long long ld8_get() {
    unsigned lo, hi;
    asm volatile("s_waitcnt vmcnt(0)\n\tv_accvgpr_read_b32 %0, a2\n\tv_accvgpr_read_b32 %1, a3" : "=v"(lo), "=v"(hi) :: "memory", "a2", "a3");
    return ((unsigned long long)hi << 32) | lo;
}
__device__ __forceinline__ void hs_issue(i32x4 rs, int off) {
    asm volatile("buffer_load_dwordx2 a[6:7], %0, %1, 0 offen sc1" :: "v"(off), "s"(rs) : "memory", "a6", "a7");
}
__device__ __forceinline__ u32x2 hs_get() {
    unsigned lo, hi;
    asm volatile("s_waitcnt vmcnt(0)\n\tv_accvgpr_read_b32 %0, a6\n\tv_accvgpr_read_b32 %1, a7" : "=v"(lo), "=v"(hi) :: "memory", "a6", "a7");
    return u32x2{lo, hi};
}
__device__ __forceinline__ void ld32_issue(i32x4 rs, int off) {
    asm volatile("buffer_load_dwordx4 a[8:11], %0, %1, 0 offen sc1\n\tbuffer_load_dwordx4 a[12:15], %0, %1, 0 offen offset:1024 sc1"
                 :: "v"(off), "s"(rs) : "memory", "a8", "a9", "a10", "a11", "a12", "a13", "a14", "a15");
}
__device__ __forceinline__ void ld32_get(u32x4& x, u32x4& y) {
    unsigned x0, x1, x2, x3, y0, y1, y2, y3;
    asm volatile("s_waitcnt vmcnt(0)\n\tv_accvgpr_read_b32 %0, a8\n\tv_accvgpr_read_b32 %1, a9\n\tv_accvgpr_read_b32 %2, a10\n\t"
                 "v_accvgpr_read_b32 %3, a11\n\tv_accvgpr_read_b32 %4, a12\n\tv_accvgpr_read_b32 %5, a13\n\t"
                 "v_accvgpr_read_b32 %6, a14\n\tv_accvgpr_read_b32 %7, a15"
                 : "=v"(x0), "=v"(x1), "=v"(x2), "=v"(x3), "=v"(y0), "=v"(y1), "=v"(y2), "=v"(y3)
                 :: "memory", "a8", "a9", "a10", "a11", "a12", "a13", "a14", "a15");
    x = u32x4{x0, x1, x2, x3};
    y = u32x4{y0, y1, y2, y3};
}
__device__ __forceinline__ float dot4_sym(float a0, float b0, float a1, float b1, float a2, float b2, float a3, float b3) {
#pragma clang fp contract(off)
    return (a0 * b0 + a1 * b1) + (a2 * b2 + a3 * b3);
}
__device__ __forceinline__ unsigned pub(float x) { const unsigned u = f2u(x); return u == EMPTY ? 0x7fc00000u : u; }

__global__ void __launch_bounds__((NC + 2) * 64, 2)
attn_decode(const uint16_t* __restrict__ k, const uint16_t* __restrict__ v, const uint16_t* __restrict__ q,
            unsigned char* __restrict__ ws, uint16_t* __restrict__ out) {
    __shared__ __attribute__((aligned(1024))) unsigned char lds_raw[LDS_BYTES];
    LAS unsigned char* lds = (LAS unsigned char*)lds_raw;
    const int bh = (blockIdx.x >> 4) * 4 + (blockIdx.x & 3), sp = (blockIdx.x >> 2) & 3, sd = sp & 1, j = sp >> 1;
    const int prober = (0x0000ff00ff000000ull >> min(blockIdx.x, 63u)) & 1;
    const int win = WIN0 + 8 * ((blockIdx.x >> 4) % NWIN);
    const int tid = threadIdx.x, lane = tid & 63, wave = __builtin_amdgcn_readfirstlane(tid >> 6);

    loader(wave, (int)(unsigned)(uintptr_t)lds, lane * 16, k + (size_t)bh * SKV * D, v + (size_t)bh * SKV * D,
           (j << 21) | (((sd ^ (bh >> 5)) & 1) << 13), prober, win * 8);
    LAS float* mx = (LAS float*)(lds + MX_OFF);

    if (wave <= NC) {
        const int w = wave - 1, h = lane >> 5;
        const u32x4 qv = *reinterpret_cast<const u32x4*>(q + (size_t)bh * D + (lane & 31) * 8);
        LAS float* sc = (LAS float*)(lds + SC_OFF) + w * 1024;
        const int rd0 = w * 1024 + lane * 16;
        constexpr float C2 = 0.0625f * 1.4426950408889634f;
        int buf = 0;
        float m = -INFINITY;
        barrier_if<false>(prober, 0);
        for (int r = 0; r < NR; ++r) {
            barrier();
            u32x4 K[8];
#pragma unroll
            for (int t = 0; t < 8; ++t) K[t] = *(const LAS u32x4*)(lds + buf + rd0 + t * 4096);
            const float s = scores(K, qv, lane) * C2;
            if (r >= N1 && (unsigned)(r - win) >= 8) m = fmaxf(m, s);
            if ((lane & 3) == 0) sc[r * 16 + h * 8 + ((lane >> 2) & 7)] = s;
            buf = (buf == (NBUF - 1) * ROUND) ? 0 : buf + ROUND;
        }
        m = wave_max(m);
        if (lane == 0) mx[w] = m;
        f32x2 acc[4];
#pragma unroll
        for (int i = 0; i < 4; ++i) acc[i] = f32x2{0.f, 0.f};
        barrier();
        {
            const f32x4 mm = *(const LAS f32x4*)mx;
            m = fmaxf(fmaxf(fmaxf(mm[0], mm[1]), fmaxf(mm[2], mm[3])), mx[8]);
            float l = 0.f;
#pragma unroll
            for (int j = 0; j < 4; ++j) {
                f32x4 sv = *(const LAS f32x4*)(sc + j * 256 + lane * 4);
#pragma unroll
                for (int e = 0; e < 4; ++e) { sv[e] = __builtin_amdgcn_exp2f(sv[e] - m); l += sv[e]; }
                *(LAS f32x4*)(sc + j * 256 + lane * 4) = sv;
            }
            l = wave_sum(l);
            if (lane == 0) mx[4 + w] = l;
            asm volatile("s_waitcnt lgkmcnt(0)" ::: "memory");
        }
#pragma nounroll
        for (int r = 0; r < NR; ++r) {
            barrier_if<false>(r, 0);
            const f32x4 p0 = *(const LAS f32x4*)(sc + r * 16 + h * 8), p1 = *(const LAS f32x4*)(sc + r * 16 + h * 8 + 4);
            auto pv = [&](int t) {
                const u32x4 V = *(const LAS u32x4*)(lds + buf + rd0 + t * 4096);
                const float p = t < 4 ? p0[t & 3] : p1[t & 3];
#pragma unroll
                for (int e = 0; e < 4; ++e)
                    acc[e] = __builtin_elementwise_fma(f32x2{p, p}, f32x2{u2f(V[e] << 16), u2f(V[e] & 0xffff0000u)}, acc[e]);
            };
#pragma unroll
            for (int t = 0; t < 6; ++t) pv(t);
            asm volatile("" : "+v"(acc[0]), "+v"(acc[1]), "+v"(acc[2]), "+v"(acc[3]));
            barrier_if<true>(r, NR - 1);
            pv(6);
            pv(7);
            buf = (buf == (NBUF - 1) * ROUND) ? 0 : buf + ROUND;
        }
        f32x4 rec;
#pragma unroll
        for (int e = 0; e < 4; ++e) {
            auto sw = __builtin_amdgcn_permlane32_swap(f2u(acc[e >> 1][e & 1]), f2u(acc[2 + (e >> 1)][e & 1]), false, false);
            rec[e] = u2f(sw[0]) + u2f(sw[1]);
        }
        *(LAS f32x4*)(sc + (lane & 31) * 8 + h * 4) = rec;
        asm volatile("s_waitcnt lgkmcnt(0)\n.Lattn_code_end1:\n\ts_barrier" ::: "memory");
        return;
    }

    const i32x4 rw = make_rsrc(ws, WS_BYTES);
    const int hs = HS_OFF + bh * HS_BYTES;
    volatile LAS unsigned* ctl = (volatile LAS unsigned*)(lds + SEL_OFF);
    ctl[0] = W_ANY - 16;
    ctl[1] = 0;
    const int die = __builtin_amdgcn_s_getreg(20 | (2 << 6));
    const int ml_off = bh * HEAD_BYTES + max(lane >> 1 & 3, 1) * REC_BYTES + 1024 + (lane & 1) * 4;
    unsigned ml = EMPTY;
    float m = 0.f, l = 0.f, w0 = 0.f, w1 = 0.f, w2 = 0.f, w3 = 0.f;
    auto weights = [&]() {
        const float m1 = u2f(__builtin_amdgcn_readlane(ml, 2)), m2 = u2f(__builtin_amdgcn_readlane(ml, 4)), m3 = u2f(__builtin_amdgcn_readlane(ml, 6));
        const float l1 = u2f(__builtin_amdgcn_readlane(ml, 3)), l2 = u2f(__builtin_amdgcn_readlane(ml, 5)), l3 = u2f(__builtin_amdgcn_readlane(ml, 7));
        const float M = fmaxf(fmaxf(m, m1), fmaxf(m2, m3));
        w0 = __builtin_amdgcn_exp2f(m - M); w1 = __builtin_amdgcn_exp2f(m1 - M); w2 = __builtin_amdgcn_exp2f(m2 - M); w3 = __builtin_amdgcn_exp2f(m3 - M);
        const float inv = 1.0f / dot4_sym(w0, l, w1, l1, w2, l2, w3, l3);
        w0 *= inv; w1 *= inv; w2 *= inv; w3 *= inv;
        asm volatile("" : "+v"(w0), "+v"(w1), "+v"(w2), "+v"(w3));
    };
    unsigned chal = 0, dec = 0, decv = 0, cseen = 0;
    auto challenge = [&]() {
        chal = __builtin_amdgcn_readfirstlane(hs_get()[0]) + 1;
        if (lane == 0) st8(rw, hs + 8 * sp, u32x2{chal, (unsigned)die});
    };
    if (sp == 0) {
#pragma unroll
        for (int s = 1; s < NSPLIT; ++s) st16(rw, bh * HEAD_BYTES + s * REC_BYTES + lane * 16, u32x4{EMPTY, EMPTY, EMPTY, EMPTY});
        st16(rw, bh * HEAD_BYTES + (lane >> 4) * REC_BYTES + 1024 + (lane & 15) * 16, u32x4{EMPTY, EMPTY, EMPTY, EMPTY});
    } else hs_issue(rw, hs + 256 + 8 * sp);
    if (prober) {
        const uint16_t* t = ((blockIdx.x >> 4) == 1 ? k : v) + (size_t)bh * SKV * D;
        const unsigned rnd = (unsigned)__builtin_amdgcn_s_memrealtime() * 0x9e3779b1u;
        const int o = (2 << 21) | ((rnd >> 11) & 0x1f5f80);
        asm volatile("global_load_dword a4, %0, %1 nt" :: "v"(o ^ 0x100000), "s"(t) : "memory", "a4");
        asm volatile("s_waitcnt vmcnt(0)" ::: "memory");
        int e = 0, hit = 0;
#pragma unroll
        for (int i = 0; i < 2; ++i) {
            unsigned long long t0, t1, t2;
            asm volatile("s_mov_b64 exec, 1\n\ts_memtime %0\n\tglobal_load_dword a4, %3, %5 nt\n\tglobal_load_dword a5, %4, %5 nt\n\t"
                         "s_waitcnt vmcnt(1)\n\ts_memtime %1\n\ts_waitcnt vmcnt(0)\n\ts_memtime %2\n\ts_mov_b64 exec, -1\n\ts_waitcnt lgkmcnt(0)"
                         : "=&s"(t0), "=&s"(t1), "=&s"(t2) : "v"(o + i * 0xa000), "v"((o + i * 0xa000) ^ 0x2000), "s"(t) : "memory", "a4", "a5");
            const int x = 2 * min((int)(t2 - t1), 500) - min((int)(t1 - t0), 2500);
            e += i ? -x : x;
            hit |= (int)(t1 - t0) < T_L2;
        }
        challenge();
        const unsigned long long now = __builtin_amdgcn_s_memrealtime();
        if (lane < 8) st16(rw, EV_OFF + (((blockIdx.x >> 5) << 3) | (blockIdx.x & 7)) * PUB_STRIDE + lane * 16,
                           u32x4{(unsigned)now, (unsigned)(now >> 32), (unsigned)(hit ? 0 : (((bh >> 5) ^ die) & 1) ? e : -e), 0});
        barrier();
    }
    unsigned code_lo, code_hi, len1, len2;
    asm volatile("s_getpc_b64 s[50:51]\n.Lattn_pc:\n\ts_sub_u32 %0, s50, .Lattn_pc-.Lattn_code_begin\n\ts_subb_u32 %1, s51, 0\n\t"
                 "s_mov_b32 %2, .Lattn_code_end1-.Lattn_code_begin\n\ts_mov_b32 %3, .Lattn_code_end2-.Lattn_code_begin"
                 : "=s"(code_lo), "=s"(code_hi), "=s"(len1), "=s"(len2) :: "s50", "s51", "scc");
    const unsigned long long code = ((unsigned long long)code_hi << 32) | code_lo;
    auto touch_code = [&]() {
        const int len = (int)max(len1, len2);
        asm volatile("global_load_dword a4, %0, %2\n\tglobal_load_dword a5, %1, %2"
                     :: "v"(min(lane * 128, len)), "v"(min(lane * 128 + 8192, len)), "s"(code) : "memory", "a4", "a5");
    };
    touch_code();

    unsigned long long tmine = 0;
    const unsigned tbase = (unsigned)__builtin_amdgcn_s_memrealtime();
    float tref = 0.f, tau = TAU0;
    int rref = 0;
    auto stamp_put = [&](int slot, unsigned long long now, int far) {
        tmine = now;
        const unsigned long long e = (now << 1) | (unsigned)far;
        if (blockIdx.x < NPUB && lane < 16)
            st8(rw, TB_OFF + (slot * NPUB + blockIdx.x) * PUB_STRIDE + lane * 8, u32x2{(unsigned)e, (unsigned)(e >> 32)});
    };
    auto stamp_fetch = [&](int slot) { ld8_issue(rw, TB_OFF + (slot * NPUB + (lane & (NPUB - 1))) * PUB_STRIDE); };
    auto stamp_use = [&](int rk) {
        const unsigned long long e = ld8_get();
        const long long d = (long long)(tmine - (e >> 1));
        bool ok = d > -2000 && d < 2000;
        if (__any(ok && (e & 1))) ok = ok && (e & 1);
        const int n = __builtin_amdgcn_readfirstlane(row_sum(ok ? 1 : 0)), s = __builtin_amdgcn_readfirstlane(row_sum(ok ? (int)d : 0));
        if (n > 0) {
            const float tm = (float)(int)((unsigned)tmine - tbase) - (float)s * __builtin_amdgcn_rcpf((float)n);
            if (rref > 0) tau = (tm - tref) * __builtin_amdgcn_rcpf((float)(rk - rref));
            tref = tm;
            rref = rk;
        }
    };
    auto reecho = [&]() {
        const unsigned c = hs_get()[0];
        if (lane >= 1 && lane < NSPLIT && c != cseen) st8(rw, hs + 256 + 8 * lane, u32x2{c, decv});
        cseen = c;
    };

    barrier();
    if (sp != 0 && !prober) challenge();
    stamp_put(0, __builtin_amdgcn_s_memrealtime(), 0);
    barrier();
    barrier();
    stamp_fetch(0);
    if (sp == 0) { ld32_issue(rw, EV_OFF + (lane & 15) * PUB_STRIDE); hs_issue(rw, hs + 8 * (lane & 3)); }
    auto decide = [&](bool last) {
        u32x4 e, unused;
        ld32_get(e, unused);
        const u32x2 c = hs_get();
        const long long d = (long long)(tmine - (((unsigned long long)e[1] << 32) | e[0]));
        const bool ok = d > -2000 && d < 2000;
        const int ek = __builtin_amdgcn_readfirstlane(row_sum(ok && !(lane & 8) ? (int)e[2] : 0));
        const int ev = __builtin_amdgcn_readfirstlane(row_sum(ok && (lane & 8) ? (int)e[2] : 0));
        const int known = abs(ek) > EMIN && abs(ev) > EMIN;
        if (!last && __popcll(__ballot(ok) & 0xffff) < (known ? 12 : 16)) {
            ld32_issue(rw, EV_OFF + (lane & 15) * PUB_STRIDE);
            hs_issue(rw, hs + 8 * (lane & 3));
            return;
        }
        const int ck = ek > 0, cv = ev > 0;
        const int die2 = __builtin_amdgcn_readlane(c[1], 2) & 1;
        const int fark = known & (((lane & 2) ? 1 ^ die2 : die) ^ ck);
        decv = (fark << (ck == cv ? 0 : 4)) | (ck << 1) | (cv << 2) | (known << 3) | 0x80000000u;
        if (lane >= 1 && lane < NSPLIT) st8(rw, hs + 256 + 8 * lane, u32x2{c[0], decv});
        cseen = c[0];
        dec = __builtin_amdgcn_readfirstlane(decv);
    };
#pragma nounroll
    for (int r = 4; r <= 18; ++r) {
        if (r <= 6) barrier();
        if (sp == 0 && dec == 0) decide(r == 18);
        else if (r > 6) break;
        if (r == 5) stamp_use(1);
    }
    hs_issue(rw, sp != 0 ? hs + 256 + 8 * sp : hs + 8 * (lane & 3));
    barrier();
    if (sp == 0) {
        reecho();
    } else {
#pragma nounroll
        for (int spin = 0; spin < (1 << 17); ++spin) {
            const u32x2 x = hs_get();
            dec = __builtin_amdgcn_readfirstlane(x[1]);
            if (__builtin_amdgcn_readfirstlane(x[0]) == chal) break;
            hs_issue(rw, hs + 256 + 8 * sp);
        }
    }
    const int cls = (sd ^ j ^ dec ^ die) & 1, known = dec >> 3 & 1;
    const int far_k = known & (cls ^ (dec >> 1)), far_v = known & (cls ^ (dec >> 2));
    const int mis = (dec >> 1 ^ dec >> 2) & 1, wn = mis ? W_NEAR2 : W_NEAR;
    const int base_k = !known ? W_ANY : far_k ? W_FAR : wn, base_v = !known ? W_ANY : far_v ? W_FAR : wn;
    ctl[0] = base_k - 16;
    ctl[1] = dec | 0x80000000u;
    LAS float* early0 = (LAS float*)(lds + SC_OFF) + (lane >> 4) * 1024 + (lane & 15) * 8;
    LAS float* early = early0 + ((dec & 16) ? win * 16 : 0);
    const int xr = (dec & 16) ? WIN0 + 8 * NWIN + 2 : XR;
    const int box = SX_OFF + (bh * NSPLIT + sp) * SX_BYTES + lane * 16, flag = bh * HEAD_BYTES + sp * REC_BYTES + 1152;
    int xs = (dec & 17) ? 0 : 3;
    auto receive = [&]() {
        if (xs == 2) {
            u32x4 x, y;
            ld32_get(x, y);
            *(LAS u32x4*)early = x;
            *(LAS u32x4*)(early + 4) = y;
            xs = 3;
        } else if (xs == 1) {
            xs = __builtin_amdgcn_readfirstlane(ld4_get()) != EMPTY ? 2 : 0;
            if (xs == 2) ld32_issue(rw, box);
        } else if (xs == 0) {
            ld4_issue(rw, flag);
            xs = 1;
        }
    };

#pragma nounroll
    for (int r = N1; r <= 2 * NR; ++r) {
        barrier();
        const unsigned long long now = __builtin_amdgcn_s_memrealtime();
        const int ph = (r - 1) & ((1 << LPER) - 1), slot = (r - 1) >> LPER;
        if (ph == PH_USE) stamp_use(r - PH_USE);
        const bool inwin = (dec & 16) && (unsigned)(r + 1 - win) < 8;
        int sel = (r + 1 < NR ? (inwin ? wn : base_k) : base_v) - 16;
        if (rref > 0) {
            const float aim = r < END_R ? 0.f : sp ? -BIAS : 3.f * BIAS;
            const float lag = (float)(int)((unsigned)now - tbase) - fmaf(tau, (float)(r - rref), tref) - aim;
            const float t1 = r < END_R ? TH1 : TE1, t2 = r < END_R ? TH2 : TE2;
            sel += (lag > t1) + (lag > t2) + (lag > TH3 && !mis) - (lag < -t1) - (lag < -t2) - (lag < -TH3 && !mis);
        }
        ctl[0] = max(sel, 0);
        if (r < NR) {
            if (r >= xr + 4) receive();
            if (sp == 0 && (r < 12 || (r & 14) == 0)) {
                if (r & 1) reecho();
                else hs_issue(rw, hs + 8 * (lane & 3));
            }
            if ((dec & 17) && r == xr) {
                st16(rw, box ^ SX_BYTES, *(const LAS u32x4*)early);
                st16(rw, (box ^ SX_BYTES) + 1024, *(const LAS u32x4*)(early + 4));
            }
            if ((dec & 17) && r == xr + 2) {
                asm volatile("s_waitcnt vmcnt(0)" ::: "memory");
                if (lane < 8) st16(rw, flag + (sd ? -REC_BYTES : REC_BYTES) + lane * 16, u32x4{1, 1, 1, 1});
            }
            if (r == NR - 1) {
#pragma nounroll
                for (int spin = 0; spin < (1 << 17) && xs != 3; ++spin) {
                    receive();
                    if (sp == 0) { hs_issue(rw, hs + 8 * (lane & 3)); reecho(); }
                }
                const f32x4 x = __builtin_elementwise_max(*(const LAS f32x4*)early0, *(const LAS f32x4*)(early0 + 4));
                const f32x4 y = __builtin_elementwise_max(*(const LAS f32x4*)(early0 + win * 16), *(const LAS f32x4*)(early0 + win * 16 + 4));
                const float m0 = wave_max(fmaxf(fmaxf(fmaxf(x[0], x[1]), fmaxf(x[2], x[3])), fmaxf(fmaxf(y[0], y[1]), fmaxf(y[2], y[3]))));
                if (lane == 0) mx[8] = m0;
            }
        }
        if (ph == 0) stamp_put(slot, now, r < NR ? far_k && !inwin : far_v);
        if (ph == PH_LD) stamp_fetch(slot);
        if (r == 71) {
            const f32x4 mm = *(const LAS f32x4*)mx, ll = *(const LAS f32x4*)(mx + 4);
            m = fmaxf(fmaxf(fmaxf(mm[0], mm[1]), fmaxf(mm[2], mm[3])), mx[8]);
            l = (ll[0] + ll[1]) + (ll[2] + ll[3]);
            if (sp != 0 && lane < 16) st8(rw, bh * HEAD_BYTES + sp * REC_BYTES + 1024 + lane * 8, u32x2{pub(m), pub(l)});
        }
        if (r == 87 && sp == 0) ld4_issue(rw, ml_off);
        if (r == 95 && sp == 0) { ml = ld4_get(); weights(); }
        if (r == 119) touch_code();
    }
    const LAS float* rec = (const LAS float*)(lds + SC_OFF) + lane * 4;
    if (sp != 0) {
        barrier();
        barrier();
        const f32x4 o = (*(const LAS f32x4*)rec + *(const LAS f32x4*)(rec + 1024)) + (*(const LAS f32x4*)(rec + 2048) + *(const LAS f32x4*)(rec + 3072));
        st16(rw, bh * HEAD_BYTES + sp * REC_BYTES + lane * 16, u32x4{pub(o[0]), pub(o[1]), pub(o[2]), pub(o[3])});
        return;
    }
    auto echo = [&]() { hs_issue(rw, hs + 8 * (lane & 3)); reecho(); };
    int passed = 0;
    if (!__all(ml != EMPTY)) {
        barrier();
        barrier();
        passed = 1;
#pragma nounroll
        for (int spin = 0; spin < (1 << 17) && !__all(ml != EMPTY); ++spin) {
            echo();
            ld4_issue(rw, ml_off);
            ml = ld4_get();
        }
        weights();
    }
    const int o1 = bh * HEAD_BYTES + REC_BYTES + lane * 16;
#define POLL(R0, R1, R2)                                                             \
    "buffer_load_dwordx4 " R0 ", %[o1], %[rw], 0 offen sc1\n\t"                      \
    "buffer_load_dwordx4 " R1 ", %[o1], %[rw], 0 offen offset:1280 sc1\n\t"          \
    "buffer_load_dwordx4 " R2 ", %[o1], %[rw], 0 offen offset:2560 sc1\n\t"
    passed = __builtin_amdgcn_readfirstlane(passed);
    barrier_if<true>(passed, 0);
#define FINISH(A, B, C)                                                              \
    "v_pk_mul_f32 v[88:89], v[" #A ":" #A "+1], %[w1]\n\tv_pk_mul_f32 v[90:91], v[" #A "+2:" #A "+3], %[w1]\n\t"   \
    "v_pk_mul_f32 v[96:97], v[" #B ":" #B "+1], %[w2]\n\tv_pk_mul_f32 v[98:99], v[" #B "+2:" #B "+3], %[w2]\n\t"   \
    "v_pk_mul_f32 v[100:101], v[" #C ":" #C "+1], %[w3]\n\tv_pk_mul_f32 v[102:103], v[" #C "+2:" #C "+3], %[w3]\n\t"
#define ALLMAX(A0,A1,A2,A3,B0,B1,B2,B3,C0,C1,C2,C3)                                  \
    "v_max3_u32 v88, v" #A0 ", v" #A1 ", v" #A2 "\n\tv_max3_u32 v89, v" #A3 ", v" #B0 ", v" #B1 "\n\t" \
    "v_max3_u32 v90, v" #B2 ", v" #B3 ", v" #C0 "\n\tv_max3_u32 v91, v" #C1 ", v" #C2 ", v" #C3 "\n\t" \
    "v_max3_u32 v88, v88, v89, v90\n\tv_max_u32 v88, v88, v91\n\t"                   \
    "v_cmp_eq_u32 vcc, -1, v88\n\ts_nop 1\n\t"
#pragma nounroll
    for (int outer = 0; outer < (1 << 11); ++outer) {
        int left = 64;
        asm volatile(
            POLL("v[64:67]", "v[68:71]", "v[72:75]")
            "s_cmp_lg_u32 %[passed], 0\n\t"
            "s_cbranch_scc1 6f\n\t"
            "s_barrier\n"
            "6:\n\t"
            "ds_read_b128 v[92:95], %[rec]\n\t"
            "ds_read_b128 v[96:99], %[rec] offset:4096\n\t"
            "ds_read_b128 v[100:103], %[rec] offset:8192\n\t"
            "ds_read_b128 v[104:107], %[rec] offset:12288\n\t"
            "s_waitcnt lgkmcnt(2)\n\t"
            "v_pk_add_f32 v[92:93], v[92:93], v[96:97]\n\tv_pk_add_f32 v[94:95], v[94:95], v[98:99]\n\t"
            "s_waitcnt lgkmcnt(0)\n\t"
            "v_pk_add_f32 v[100:101], v[100:101], v[104:105]\n\tv_pk_add_f32 v[102:103], v[102:103], v[106:107]\n\t"
            "v_pk_add_f32 v[92:93], v[92:93], v[100:101]\n\tv_pk_add_f32 v[94:95], v[94:95], v[102:103]\n\t"
            "v_mul_f32 v92, %[w0], v92\n\tv_mul_f32 v93, %[w0], v93\n\tv_mul_f32 v94, %[w0], v94\n\tv_mul_f32 v95, %[w0], v95\n"
            "1:\n\t"
            POLL("v[76:79]", "v[80:83]", "v[84:87]")
            "s_waitcnt vmcnt(3)\n\t"
            ALLMAX(64,65,66,67,68,69,70,71,72,73,74,75)
            "s_cbranch_vccz 2f\n\t"
            POLL("v[64:67]", "v[68:71]", "v[72:75]")
            "s_waitcnt vmcnt(3)\n\t"
            ALLMAX(76,77,78,79,80,81,82,83,84,85,86,87)
            "s_cbranch_vccz 3f\n\t"
            "s_sub_u32 %[left], %[left], 1\n\t"
            "s_cmp_lg_u32 %[left], 0\n\t"
            "s_cbranch_scc1 1b\n\t"
            "s_branch 5f\n"
            "2:\n\t"
            FINISH(64, 68, 72)
            "s_branch 4f\n"
            "3:\n\t"
            FINISH(76, 80, 84)
            "4:\n\t"
            "v_pk_add_f32 v[88:89], v[88:89], v[92:93]\n\tv_pk_add_f32 v[90:91], v[90:91], v[94:95]\n\t"
            "v_pk_add_f32 v[96:97], v[96:97], v[100:101]\n\tv_pk_add_f32 v[98:99], v[98:99], v[102:103]\n\t"
            "v_pk_add_f32 v[88:89], v[88:89], v[96:97]\n\tv_pk_add_f32 v[90:91], v[90:91], v[98:99]\n\t"
            "v_cvt_pk_bf16_f32 v88, v88, v89\n\t"
            "v_cvt_pk_bf16_f32 v89, v90, v91\n\t"
            "global_store_dwordx2 %[oo], v[88:89], %[op]\n"
            ".Lattn_code_end2:\n"
            "5:"
            : [left] "+s"(left)
            : [o1] "v"(o1), [rw] "s"(rw), [w0] "v"(w0), [w1] "v"(f32x2{w1, w1}), [w2] "v"(f32x2{w2, w2}), [w3] "v"(f32x2{w3, w3}), [passed] "s"(passed),
              [rec] "v"((int)(unsigned)(uintptr_t)rec), [oo] "v"(lane * 8), [op] "s"(out + bh * D)
            : "memory", "scc", "vcc", "v64", "v65", "v66", "v67", "v68", "v69", "v70", "v71", "v72", "v73", "v74", "v75", "v76", "v77",
              "v78", "v79", "v80", "v81", "v82", "v83", "v84", "v85", "v86", "v87", "v88", "v89", "v90", "v91", "v92", "v93",
              "v94", "v95", "v96", "v97", "v98", "v99", "v100", "v101", "v102", "v103", "v104", "v105", "v106", "v107");
        if (left != 0) break;
        passed = 1;
        echo();
    }
}

extern "C" void kernel_launch(void* const* d_in, const int* in_sizes, int n_in,
                              void* d_out, int out_size, void* d_ws, size_t ws_size,
                              hipStream_t stream) {
    const uint16_t* q = (const uint16_t*)d_in[0];
    const uint16_t* k = (const uint16_t*)d_in[1];
    const uint16_t* v = (const uint16_t*)d_in[2];
    attn_decode<<<NBH * NSPLIT, (NC + 2) * 64, 0, stream>>>(k, v, q, (unsigned char*)d_ws, (uint16_t*)d_out);
}
```
